```python
import torch

def _softmax(x, axis=-1):
    return torch.softmax(x, dim=axis)
def _gelu(x):
    return torch.nn.functional.gelu(x, approximate="tanh")
def _silu(x):
    return torch.nn.functional.silu(x)
def _sigmoid(x):
    return torch.sigmoid(x)
def _layernorm(x, axis=-1, eps=1e-5):
    m = x.mean(dim=axis, keepdim=True)
    v = x.var(dim=axis, keepdim=True, unbiased=False)
    return (x - m) / torch.sqrt(v + eps)
def _scatter_add(tgt, idx, src, axis=0):
    return tgt.clone().index_add_(axis, idx, src)
def _segment_sum(src, seg, n):
    out = torch.zeros((n,) + src.shape[1:], dtype=src.dtype)
    return out.index_add_(0, seg, src)
def _pad(x, pw):
    flat = tuple(v for pair in reversed(pw) for v in pair)
    return torch.nn.functional.pad(x, flat)

kernel_name = 'concat_heads__ssm_conv_s5__linear_attn_block__prompt'

def setup_inputs():

    _w = (lambda a: (lambda t: t.to(torch.bfloat16) if t.is_floating_point() else t)(torch.as_tensor(a)))
    def _A_setup():
        B, S, D = 1, 2048, 512
        P = D
        Lambda_re = -0.5 * (lambda _x: torch.exp(torch.as_tensor(_x)))((lambda *s: torch.randn(*s, dtype=torch.bfloat16) * 0.2)(P))
        Lambda_im = (lambda *s: torch.randn(*s, dtype=torch.bfloat16) * 0.8)(P) * 1.0
        B_re = (lambda *s: torch.randn(*s, dtype=torch.bfloat16) * 0.2)(P, D) * (D ** -0.5)
        B_im = (lambda *s: torch.randn(*s, dtype=torch.bfloat16) * 0.2)(P, D) * (D ** -0.5)
        C_re = (lambda *s: torch.randn(*s, dtype=torch.bfloat16) * 0.2)(D, P) * (P ** -0.5)
        C_im = (lambda *s: torch.randn(*s, dtype=torch.bfloat16) * 0.2)(D, P) * (P ** -0.5)
        log_dt = (lambda *s: torch.randn(*s, dtype=torch.bfloat16) * 0.2)(P) * 0.3 - 2.0
        Dskip = (lambda *s: torch.randn(*s, dtype=torch.bfloat16) * 0.05)(D)
        return {
            "x": _w((lambda *s: torch.randn(*s, dtype=torch.bfloat16) * 0.05)(B, S, D)),
            "Lambda_re": _w(Lambda_re),
            "Lambda_im": _w(Lambda_im),
            "B_re": _w(B_re),
            "B_im": _w(B_im),
            "C_re": _w(C_re),
            "C_im": _w(C_im),
            "log_dt": _w(log_dt),
            "Dskip": _w(Dskip),
        }
    _A_in = _A_setup()

    def _B_setup():
        B, S, D = 1, 2048, 512
        return {
            "x": _w((lambda *s: torch.randn(*s, dtype=torch.bfloat16) * 0.05)(B, S, D)),
            "wqkv": _w((lambda *s: torch.randn(*s, dtype=torch.bfloat16) * 0.4)(D, 3 * D)),
            "wo": _w((lambda *s: torch.randn(*s, dtype=torch.bfloat16) * 0.05)(D, D)),
        }
    _B_in = _B_setup()

    return {
        "A_x": _A_in["x"],
        "A_Lambda_re": _A_in["Lambda_re"],
        "A_Lambda_im": _A_in["Lambda_im"],
        "A_B_re": _A_in["B_re"],
        "A_B_im": _A_in["B_im"],
        "A_C_re": _A_in["C_re"],
        "A_C_im": _A_in["C_im"],
        "A_log_dt": _A_in["log_dt"],
        "A_Dskip": _A_in["Dskip"],
        "B_x": _B_in["x"],
        "B_wqkv": _B_in["wqkv"],
        "B_wo": _B_in["wo"],
    }

def reference(A_x, A_Lambda_re, A_Lambda_im, A_B_re, A_B_im, A_C_re, A_C_im, A_log_dt, A_Dskip, B_x, B_wqkv, B_wo):
    def _A_ref(x, Lambda_re, Lambda_im, B_re, B_im, C_re, C_im, log_dt, Dskip):
        Bsz, L, H = x.shape
        Lam = Lambda_re + 1j * Lambda_im
        Bmat = B_re + 1j * B_im
        Cmat = C_re + 1j * C_im
        dt = torch.exp(log_dt)
        Lam_bar = torch.exp(dt * Lam)
        B_bar = ((Lam_bar - 1.0) / Lam)[:, None] * Bmat
        Bu = torch.einsum("bld,pd->blp", x + 0j, B_bar)
        h = torch.zeros_like(Bu[:, 0, :])
        hs = []
        for t in range(L):
            h = Lam_bar[None, :] * h + Bu[:, t, :]
            hs.append(h)
        hseq = torch.stack(hs, 1)
        y = 2.0 * torch.einsum("blp,dp->bld", hseq, Cmat).real + x * Dskip[None, None, :]
        return y

    def _B_ref(x, wqkv, wo):
        B, S, D = x.shape
        H = 32; Dh = D // H
        qkv = (lambda x, *d: x.permute(*d))((x @ wqkv).reshape(B, S, 3, H, Dh), 2, 0, 3, 1, 4)
        q, k, v = qkv[0], qkv[1], qkv[2]
        phi = lambda t: torch.where(t > 0, t + 1, torch.exp(t)) + 1e-6
        pq, pk = phi(q), phi(k)
        kv = torch.einsum("bhsd,bhse->bhde", pk, v)
        z = torch.einsum("bhsd->bhd", pk)
        num = torch.einsum("bhqd,bhde->bhqe", pq, kv)
        den = torch.einsum("bhqd,bhd->bhq", pq, z)[..., None] + 1e-6
        o = num / den
        return (lambda x, *d: x.permute(*d))(o, 0, 2, 1, 3).reshape(B, S, D) @ wo

    a = _A_ref(A_x, A_Lambda_re, A_Lambda_im, A_B_re, A_B_im, A_C_re, A_C_im, A_log_dt, A_Dskip)
    b = _B_ref(B_x, B_wqkv, B_wo)
    return torch.concatenate([a, b], axis=-1)
```

```cpp
#include <hip/hip_runtime.h>
#include <hip/hip_bf16.h>

typedef __bf16 bf16x2_t __attribute__((ext_vector_type(2)));
typedef __bf16 bf16x8_t __attribute__((ext_vector_type(8)));
typedef short s16x4 __attribute__((ext_vector_type(4)));
typedef float f32x2 __attribute__((ext_vector_type(2)));
typedef float f32x4 __attribute__((ext_vector_type(4)));
typedef float f32x16 __attribute__((ext_vector_type(16)));
typedef unsigned u32x4 __attribute__((ext_vector_type(4)));
typedef unsigned u32x2 __attribute__((ext_vector_type(2)));
typedef unsigned short u16;
#define DI __device__ __forceinline__

DI unsigned pk_bf16(float a, float b) {
  f32x2 v = {a, b};
  bf16x2_t r = __builtin_convertvector(v, bf16x2_t);
  return __builtin_bit_cast(unsigned, r);
}
DI float bf_lo(unsigned u) { return __builtin_bit_cast(float, u << 16); }
DI float bf_hi(unsigned u) { return __builtin_bit_cast(float, u & 0xffff0000u); }
DI float bf2f(u16 v) { return __builtin_bit_cast(float, (unsigned)v << 16); }
DI float rbf(float a) { return bf_lo(pk_bf16(a, 0.f)); }
DI f32x16 mfma32(u32x4 a, u32x4 b, f32x16 c) {
  return __builtin_amdgcn_mfma_f32_32x32x16_bf16(__builtin_bit_cast(bf16x8_t, a), __builtin_bit_cast(bf16x8_t, b), c, 0, 0, 0);
}
DI f32x4 mfma16(u32x4 a, u32x4 b, f32x4 c) {
  return __builtin_amdgcn_mfma_f32_16x16x32_bf16(__builtin_bit_cast(bf16x8_t, a), __builtin_bit_cast(bf16x8_t, b), c, 0, 0, 0);
}
typedef __attribute__((address_space(3))) s16x4* lds_s16x4_p;
DI u32x2 tr_read(const char* p) {
  s16x4 a = __builtin_amdgcn_ds_read_tr16_b64_v4i16((lds_s16x4_p)p);
  return __builtin_bit_cast(u32x2, a);
}
DI u32x4 tr_frag(const char* p, int stride4) {
  const u32x2 a = tr_read(p), b = tr_read(p + stride4);
  u32x4 f = {a[0], a[1], b[0], b[1]};
  return f;
}
DI void glds16(unsigned voff, const void* sbase, unsigned lds_dst) {
  asm volatile("s_mov_b32 m0, %2\n\ts_nop 0\n\tglobal_load_lds_dwordx4 %0, %1"
               :: "v"(voff), "s"(sbase), "s"(lds_dst) : "memory");
}
#define WAIT_V(n) asm volatile("s_waitcnt vmcnt(" #n ")" ::: "memory")
#define WAIT_L0() asm volatile("s_waitcnt lgkmcnt(0)" ::: "memory")
#define BAR() do { asm volatile("" ::: "memory"); __builtin_amdgcn_s_barrier(); asm volatile("" ::: "memory"); } while (0)
#define SBAR() do { __builtin_amdgcn_sched_barrier(0); BAR(); __builtin_amdgcn_sched_barrier(0); } while (0)
#define INTERLEAVE(n) do { _Pragma("unroll") for (int q_ = 0; q_ < (n); ++q_) { __builtin_amdgcn_sched_group_barrier(0x8, 1, 0); \
    __builtin_amdgcn_sched_group_barrier(0x100, 1, 0); } __builtin_amdgcn_sched_barrier(0); } while (0)
template <class T> DI void st16(void* p, T v) {
  asm volatile("global_store_dwordx4 %0, %1, off sc1\n\ts_nop 1" :: "v"(p), "v"(v) : "memory");
}
template <class T> DI void ws16(void* p, T v) { st16(p, v); }
template <class T> DI void ws8(void* p, T v) { *(T*)p = v; }
DI f32x2 pfma(f32x2 a, f32x2 b, f32x2 c) { return __builtin_elementwise_fma(a, b, c); }

constexpr size_t WS_BU  = 0;
constexpr size_t WS_PQF = 4u << 20;
constexpr size_t WS_CF  = 6u << 20;
constexpr size_t WS_WOF = 7u << 20;
constexpr size_t WS_PKV = (7u << 20) + (512u << 10);
constexpr size_t WS_PZ  = (7u << 20) + (768u << 10);
constexpr size_t WS_REC = 8u << 20;
constexpr size_t WS_TOT = (8u << 20) + (512u << 10);
constexpr size_t WS_LAM = (8u << 20) + (544u << 10);

struct Params {
  const u16* ax; const u16* lre; const u16* lim; const u16* bre; const u16* bim; const u16* cre; const u16* cim;
  const u16* ldt; const u16* dskip; const u16* bx; const u16* wqkv; const u16* wo;
  float* out; char* ws;
};

DI f32x2 cmul(f32x2 a, f32x2 b) { f32x2 r; r[0] = a[0] * b[0] - a[1] * b[1]; r[1] = a[0] * b[1] + a[1] * b[0]; return r; }
DI f32x2 cfma(f32x2 a, f32x2 b, f32x2 c) {
  f32x2 r;
  r[0] = fmaf(a[0], b[0], fmaf(-a[1], b[1], c[0]));
  r[1] = fmaf(a[0], b[1], fmaf(a[1], b[0], c[1]));
  return r;
}
DI unsigned phi_pk(float a, float b) {
  const float ea = __builtin_amdgcn_exp2f(fminf(a * 1.4426950408889634f, 0.f));
  const float eb = __builtin_amdgcn_exp2f(fminf(b * 1.4426950408889634f, 0.f));
  return pk_bf16((fmaxf(a, 0.f) + 1e-6f) + ea, (fmaxf(b, 0.f) + 1e-6f) + eb);
}

constexpr int K1_XB = 32768;
constexpr int K1_STG = K1_XB + 12288;
constexpr int K1_EXTRA = 3 * K1_STG;
constexpr int K1_LDS = K1_EXTRA + 17408;

template <bool ISB> DI int xswz_of(int row) { return ISB ? ((row >> 1) & 7) : (((row >> 1) & 1) | (((row >> 3) & 3) << 1)); }

template <bool ISB>
DI void k1_loader(const Params& p, char* sm, int R, int Cb, int koff, int w) {
  const int tid = threadIdx.x, lane = tid & 63;
  char* ex = sm + K1_EXTRA;
  const unsigned smb = __builtin_amdgcn_readfirstlane((unsigned)(size_t)sm);
  const u16* X = (ISB ? p.bx : p.ax) + (size_t)R * 256 * 512;
  constexpr int NW = ISB ? 3 : 2;
  unsigned wv[NW];
  const u16* W;
  if constexpr (!ISB) {
    W = ((w < 2) ? p.bre : p.bim) + (size_t)32 * Cb * 512;
#pragma unroll
    for (int c = 0; c < 2; ++c) {
      const int n = (w * 2 + c) * 8 + (lane >> 3), slot = lane & 7;
      wv[c] = (n & 31) * 1024 + ((slot ^ ((n >> 1) & 7)) * 16);
    }
  } else {
    W = p.wqkv + 32 * Cb;
#pragma unroll
    for (int c = 0; c < 3; ++c) {
      const int i = (w * 3 + c) * 64 + lane;
      const int seg = i >> 8, k = (i >> 2) & 63, cc = (i & 3) ^ (((k >> 3) & 1) << 1);
      wv[c] = k * 3072 + seg * 1024 + cc * 16;
    }
  }
  unsigned xv[8];
#pragma unroll
  for (int c = 0; c < 8; ++c) {
    const int row = (w * 8 + c) * 8 + (lane >> 3), slot = lane & 7;
    xv[c] = row * 1024 + ((slot ^ xswz_of<ISB>(row)) * 16);
  }
  auto stage = [&](int t, int slot) {
    const int kt = (t + koff) & 7;
    const unsigned sb = smb + slot * K1_STG;
    const u16* xb = X + kt * 64;
    const u16* wb = W + (ISB ? (size_t)kt * 64 * 1536 : (size_t)kt * 64);
#pragma unroll
    for (int c = 0; c < NW; ++c) glds16(wv[c], wb, sb + K1_XB + (w * NW + c) * 1024);
#pragma unroll
    for (int c = 0; c < 8; ++c) glds16(xv[c], xb, sb + (w * 8 + c) * 1024);
  };
  stage(0, 0);
  int slot = 1;
#pragma unroll 1
  for (int t = 1; t < 8; ++t) {
    stage(t, slot);
    slot = slot == 2 ? 0 : slot + 1;
    if constexpr (ISB) WAIT_V(11); else WAIT_V(10);
    BAR();
  }
  WAIT_V(0);
  BAR();
  if constexpr (ISB) {
    BAR();
    const float* red = (const float*)ex;
    f32x2 s = {0.f, 0.f};
#pragma unroll
    for (int ww = 0; ww < 4; ++ww) s += *(const f32x2*)(red + ww * 544 + tid * 2);
    *(unsigned*)(p.ws + WS_PKV + ((size_t)(R * 32 + 2 * Cb) * 256 + tid * 2) * 2) = pk_bf16(s[0], s[1]);
    if (tid < 32) {
      float z = 0.f;
#pragma unroll
      for (int ww = 0; ww < 4; ++ww) z += red[ww * 544 + 512 + tid];
      ((float*)(p.ws + WS_PZ))[(size_t)(R * 32 + 2 * Cb) * 16 + tid] = z;
    }
  }
}

constexpr int HWAIT = 7;
DI void k1_helper(const Params& p, char* sm, int R, int Cb, int awg, int hw) {
  const int tid = threadIdx.x, lane = tid & 63;
  const int r = lane & 31, h = lane >> 5;
  const int ht = tid - 768;
  char* ex = sm + K1_EXTRA;
  const int ch = 32 * Cb + r;
  f32x2 L1 = {0.f, 0.f}, L8 = {0.f, 0.f};
  if (hw == 0) {
    const float lr = bf2f(p.lre[ch]), li = bf2f(p.lim[ch]);
    const float dt = rbf(expf(bf2f(p.ldt[ch])));
    const float ea = expf(dt * lr);
    float sn, cs;
    sincosf(dt * li, &sn, &cs);
    L1[0] = ea * cs; L1[1] = ea * sn;
    f32x2 coef;
    const float nr = L1[0] - 1.f, ni = L1[1];
    const float inv = 1.f / (lr * lr + li * li);
    coef[0] = 2.f * (nr * lr + ni * li) * inv;
    coef[1] = 2.f * (ni * lr - nr * li) * inv;
    const f32x2 L2 = cmul(L1, L1), L4 = cmul(L2, L2);
    L8 = cmul(L4, L4);
    if (h == 0) {
      f32x4 a = {L1[0], L1[1], L2[0], L2[1]};
      *(f32x4*)(ex + r * 32) = a;
      *(f32x2*)(ex + r * 32 + 16) = coef;
    }
    WAIT_L0();
  }
  BAR();
  {
    u32x4 cfv[2]; u16 wo4[2][4];
#pragma unroll
    for (int i = 0; i < 2; ++i) {
      const int g = awg * 512 + i * 256 + ht;
      const int plane = g >> 15, nblk = (g >> 11) & 15, ks = (g >> 7) & 15, nt = (g >> 6) & 1;
      const u16* C = plane ? p.cim : p.cre;
      cfv[i] = *(const u32x4*)(C + (size_t)(32 * nblk + 16 * nt + (lane & 15)) * 512 + 32 * ks + 8 * (lane >> 4));
      const int rr = g & 31, hh = (g >> 5) & 1, jq = (g >> 6) & 1, cb = (g >> 7) & 1, head = (g >> 8) & 31, nb = g >> 13;
      const u16* s = p.wo + (size_t)(16 * head + 4 * hh + 8 * jq) * 512 + nb * 64 + cb * 32 + rr;
      wo4[i][0] = s[0]; wo4[i][1] = s[512]; wo4[i][2] = s[1024]; wo4[i][3] = s[1536];
    }
    __builtin_amdgcn_sched_barrier(0);
#pragma unroll
    for (int t = 1; t <= HWAIT; ++t) BAR();
    __builtin_amdgcn_sched_barrier(0);
#pragma unroll
    for (int i = 0; i < 2; ++i) {
      const int g = awg * 512 + i * 256 + ht;
      if (g >> 15) { cfv[i][0] ^= 0x80008000u; cfv[i][1] ^= 0x80008000u; cfv[i][2] ^= 0x80008000u; cfv[i][3] ^= 0x80008000u; }
      ws16(p.ws + WS_CF + (size_t)g * 16, cfv[i]);
      const int rr = g & 31, hh = (g >> 5) & 1, jq = (g >> 6) & 1, cb = (g >> 7) & 1, head = (g >> 8) & 31, nb = g >> 13;
      u32x2 o; o[0] = (unsigned)wo4[i][0] | ((unsigned)wo4[i][1] << 16); o[1] = (unsigned)wo4[i][2] | ((unsigned)wo4[i][3] << 16);
      ws8(p.ws + WS_WOF + ((size_t)((((nb * 32 + head) * 2 + cb) * 64 + rr + 32 * hh) * 8 + 4 * jq)) * 2, o);
    }
  }
  if (hw != 0) return;
  const f32x2 L16 = cmul(L8, L8), L32 = cmul(L16, L16);
  if (R == 0 && h == 0) {
    const f32x2 L64 = cmul(L32, L32), L128 = cmul(L64, L64), L256 = cmul(L128, L128);
    f32x4 a = {L1[0], L1[1], L256[0], L256[1]};
    *(f32x4*)(p.ws + WS_LAM + (size_t)ch * 16) = a;
  }
#pragma unroll
  for (int t = HWAIT + 1; t < 8; ++t) BAR();
  BAR();
  if (h == 0) {
    const f32x2 L64 = cmul(L32, L32);
    f32x2 s = {0.f, 0.f}, P = {1.f, 0.f};
#pragma unroll
    for (int q = 0; q < 4; ++q) {
      f32x4 rec = {s[0], s[1], P[0], P[1]};
      *(f32x4*)(p.ws + WS_REC + ((size_t)(R * 4 + q) * 512 + ch) * 16) = rec;
      P = cmul(P, L64);
#pragma unroll
      for (int i = 0; i < 8; ++i) s = cfma(L8, s, *(const f32x2*)(ex + 1024 + ((q * 8 + i) * 32 + r) * 8));
    }
    *(f32x2*)(p.ws + WS_TOT + ((size_t)((R >> 1) * 512 + ch) * 2 + (R & 1)) * 8) = s;
  }
}

template <bool ISB>
DI void k1_cons(const Params& p, char* sm, int R, int Cb, int c) {
  constexpr int NM = 4, NN = ISB ? 3 : 2;
  const int tid = threadIdx.x, lane = tid & 63;
  const int i16 = lane & 15, g = lane >> 4;
  const int rb = c >> 1, hf = c & 1;
  char* ex = sm + K1_EXTRA;
  int xo[NM];
#pragma unroll
  for (int m = 0; m < NM; ++m) {
    const int row = 64 * rb + (ISB ? (16 * m + i16) : (32 * (m >> 1) + 8 * (i16 >> 2) + 4 * (m & 1) + (i16 & 3)));
    xo[m] = row * 128 + ((g ^ xswz_of<ISB>(row)) * 16);
  }
  const int wo = K1_XB + hf * 2048 + i16 * 128 + ((g ^ ((i16 >> 1) & 7)) * 16);
  const int tro = K1_XB + ((8 * g + (i16 >> 2)) * 64 + (((lane & 3) >> 1) ^ ((g & 1) << 1)) * 16 + (lane & 1) * 8 ^ (hf * 32));

  struct Frag { u32x4 x[NM]; u32x4 w[NN]; };
  auto rd = [&](Frag& f, const char* sb, int ks) {
#pragma unroll
    for (int m = 0; m < NM; ++m) f.x[m] = *(const u32x4*)(sb + (ks ? (xo[m] ^ 64) : xo[m]));
#pragma unroll
    for (int n = 0; n < NN; ++n) {
      if constexpr (!ISB) f.w[n] = *(const u32x4*)(sb + (ks ? (wo ^ 64) : wo) + n * 4096);
      else f.w[n] = tr_frag(sb + tro + n * 4096 + ks * 2048, 256);
    }
  };
  f32x4 a[NM][NN] = {};
  auto mm = [&](const Frag& f) {
#pragma unroll
    for (int n = 0; n < NN; ++n)
#pragma unroll
      for (int m = 0; m < NM; ++m) {
        if (ISB && n == 0) a[m][n] = mfma16(f.w[n], f.x[m], a[m][n]);
        else a[m][n] = mfma16(f.x[m], f.w[n], a[m][n]);
      }
  };
  Frag fa, fb;
  BAR();
  int so = 0;
  rd(fa, sm, 0);
#pragma unroll 1
  for (int t = 0; t < 7; ++t) {
    rd(fb, sm + so, 1);
    mm(fa);
    INTERLEAVE(NM * NN);
    so = so == 2 * K1_STG ? 0 : so + K1_STG;
    WAIT_L0(); BAR();
    rd(fa, sm + so, 0);
    mm(fb);
    INTERLEAVE(NM * NN);
  }
  rd(fb, sm + so, 1);
  mm(fa);
  mm(fb);

  if constexpr (!ISB) {
    const int cl = 16 * hf + i16;
    const f32x4 LL = *(const f32x4*)(ex + cl * 32);
    const f32x2 coef = *(const f32x2*)(ex + cl * 32 + 16);
    const f32x2 cr = {coef[0], coef[0]}, ci = {coef[1], coef[1]}, nci = {-coef[1], -coef[1]};
    const f32x2 l2r = {LL[2], LL[2]}, l2i = {LL[3], LL[3]}, nl2i = {-LL[3], -LL[3]};
#pragma unroll
    for (int half = 0; half < 2; ++half) {
      const int t0 = R * 256 + 64 * rb + 32 * half + 8 * g;
      f32x2 er = {0.f, 0.f}, ei = {0.f, 0.f};
      char* dst = p.ws + WS_BU + ((size_t)(t0 / 4) * 512 + 32 * Cb + cl) * 16;
#pragma unroll
      for (int m = 0; m < 2; ++m) {
        u32x4 o;
#pragma unroll
        for (int j = 0; j < 2; ++j) {
          const f32x2 ur = {a[2 * half + m][0][2 * j], a[2 * half + m][0][2 * j + 1]};
          const f32x2 ui = {a[2 * half + m][1][2 * j], a[2 * half + m][1][2 * j + 1]};
          const f32x2 br = pfma(nci, ui, cr * ur);
          const f32x2 bi = pfma(ci, ur, cr * ui);
          const f32x2 er2 = pfma(l2r, er, pfma(nl2i, ei, br));
          ei = pfma(l2r, ei, pfma(l2i, er, bi));
          er = er2;
          o[2 * j] = pk_bf16(br[0], bi[0]);
          o[2 * j + 1] = pk_bf16(br[1], bi[1]);
        }
        ws16(dst + m * 8192, o);
      }
      f32x2 L = {LL[0], LL[1]}, ev = {er[0], ei[0]}, od = {er[1], ei[1]};
      *(f32x2*)(ex + 1024 + ((4 * (2 * rb + half) + g) * 32 + cl) * 8) = cfma(L, ev, od);
    }
    WAIT_L0();
    BAR();
  } else {
    const u32x4 ones = {0x3f803f80u, 0x3f803f80u, 0x3f803f80u, 0x3f803f80u};
    float* red = (float*)ex + rb * 544;
    const int hh = g & 1, jq = g >> 1;
    f32x4 kv = {}, zz = {};
#pragma unroll
    for (int mp = 0; mp < 2; ++mp) {
      u32x4 pk, vv;
#pragma unroll
      for (int m = 0; m < 2; ++m)
#pragma unroll
        for (int j = 0; j < 2; ++j) {
          pk[2 * m + j] = phi_pk(a[2 * mp + m][1][2 * j], a[2 * mp + m][1][2 * j + 1]);
          vv[2 * m + j] = pk_bf16(a[2 * mp + m][2][2 * j], a[2 * mp + m][2][2 * j + 1]);
        }
      kv = mfma16(pk, vv, kv);
      zz = mfma16(pk, ones, zz);
    }
    *(f32x4*)(red + ((hf * 16 + i16) * 2 + hh) * 8 + 4 * jq) = kv;
    if (i16 == 0) *(f32x4*)(red + 512 + hf * 16 + hh * 8 + 4 * jq) = zz;
    WAIT_L0();
    BAR();
    char* dq = p.ws + WS_PQF + ((size_t)((R * 8 + 2 * rb) * 32 + 2 * Cb + hf) * 64 + i16 + 32 * hh) * 16 + 8 * jq;
#pragma unroll
    for (int m = 0; m < 4; ++m) {
      u32x2 f;
      f[0] = phi_pk(a[m][0][0], a[m][0][1]);
      f[1] = phi_pk(a[m][0][2], a[m][0][3]);
      ws8(dq + (m >> 1) * 32768 + (m & 1) * 256, f);
    }
  }
}

__global__ __launch_bounds__(1024, 4) void k1(Params p) {
  extern __shared__ __attribute__((aligned(16))) char sm[];
  const int bid = blockIdx.x, xcd = bid & 7, j = bid >> 3;
  constexpr int ROTG = 4;
  const int jj = j >> 1, rb = jj >> 3;
  const int R = (xcd >> 1) * 2 + rb, cb = (xcd & 1) * 8 + (jj & 7);
  const int koff = ((jj + (ROTG / 2) * rb) & (ROTG - 1)) * (8 / ROTG);
  const bool isA = (j & 1) == 0;
  const int awg = xcd * 16 + jj;
  const int w = __builtin_amdgcn_readfirstlane(threadIdx.x >> 6);
  if (isA) {
    if (w < 4) k1_loader<false>(p, sm, R, cb, koff, w);
    else if (w < 12) k1_cons<false>(p, sm, R, cb, w - 4);
    else k1_helper(p, sm, R, cb, awg, w - 12);
  } else {
    if (w < 4) k1_loader<true>(p, sm, R, cb, koff, w);
    else if (w < 12) k1_cons<true>(p, sm, R, cb, w - 4);
  }
}

constexpr int OSTR = 528;
constexpr int OSTRB = 272;
constexpr int K2_LDS = 131072;

DI void k2_a_epi(const Params& p, char* sm, int t0, int cbk, const u32x2 (&xs)[2], u32x2 dsk) {
  const int tid = threadIdx.x;
  const float d0 = bf_lo(dsk[0]), d1 = bf_hi(dsk[0]), d2 = bf_lo(dsk[1]), d3 = bf_hi(dsk[1]);
#pragma unroll
  for (int i = 0; i < 2; ++i) {
    const int row = 32 * i + (tid >> 5);
    const char* ib = sm + row * OSTR + (tid & 31) * 16;
    f32x4 o = *(const f32x4*)(ib) + *(const f32x4*)(ib + 64 * OSTR);
    o[0] = fmaf(bf_lo(xs[i][0]), d0, o[0]); o[1] = fmaf(bf_hi(xs[i][0]), d1, o[1]);
    o[2] = fmaf(bf_lo(xs[i][1]), d2, o[2]); o[3] = fmaf(bf_hi(xs[i][1]), d3, o[3]);
    st16(p.out + (size_t)(t0 + row) * 1024 + cbk * 128 + (tid & 31) * 4, o);
  }
}
constexpr int BLA = 8;
constexpr int CLA = 8;
DI void k2_a_scan(const Params& p, char* sm, int jb, int cbk) {
  const int tid = threadIdx.x;
  const int t0 = jb * 64;
  const int ch = tid;
  const int c = jb >> 2, q = jb & 3;
  const f32x4 lam = *(const f32x4*)(p.ws + WS_LAM + (size_t)ch * 16);
  f32x4 tot[4];
#pragma unroll
  for (int i = 0; i < 4; ++i) tot[i] = *(const f32x4*)(p.ws + WS_TOT + ((size_t)i * 512 + ch) * 16);
  const f32x4 rec = *(const f32x4*)(p.ws + WS_REC + ((size_t)(c * 4 + q) * 512 + ch) * 16);
  u32x4 bu[16];
  const char* bub = p.ws + WS_BU + ((size_t)(t0 / 4) * 512 + ch) * 16;
#pragma unroll
  for (int i = 0; i < BLA; ++i) bu[i] = *(const u32x4*)(bub + i * 8192);
  __builtin_amdgcn_sched_barrier(0);
  const f32x2 L = {lam[0], lam[1]}, L256 = {lam[2], lam[3]};
  f32x2 st = {0.f, 0.f};
#pragma unroll
  for (int i = 0; i < 7; ++i) if (i < c) { const f32x2 tv = {tot[i >> 1][2 * (i & 1)], tot[i >> 1][2 * (i & 1) + 1]}; st = cfma(L256, st, tv); }
  { const f32x2 sq = {rec[0], rec[1]}, Lq = {rec[2], rec[3]}; st = cfma(Lq, st, sq); }
  const int sw = (((ch >> 1) & 1) << 2) | ((((ch >> 2) ^ (ch >> 3)) & 1) << 1) | (ch & 1);
  const f32x2 lr2 = {L[0], L[0]}, li2 = {-L[1], L[1]};
  char* hrow = sm + (ch ^ ((jb & 3) << 7)) * 128;
#pragma unroll
  for (int i = 0; i < 8; ++i) {
    if (2 * i + BLA < 16) { bu[2 * i + BLA] = *(const u32x4*)(bub + (2 * i + BLA) * 8192); bu[2 * i + BLA + 1] = *(const u32x4*)(bub + (2 * i + BLA + 1) * 8192); }
    __builtin_amdgcn_sched_barrier(0);
    if (2 * i + BLA == 14) SBAR();
    float hr[8], hi[8];
#pragma unroll
    for (int j = 0; j < 8; ++j) {
      const unsigned u = bu[2 * i + (j >> 2)][j & 3];
      const f32x2 b = {bf_lo(u), bf_hi(u)};
      const f32x2 sx = {st[1], st[0]};
      st = pfma(li2, sx, pfma(lr2, st, b));
      hr[j] = st[0]; hi[j] = st[1];
    }
    u32x4 vr, vi;
#pragma unroll
    for (int j = 0; j < 4; ++j) { vr[j] = pk_bf16(hr[2 * j], hr[2 * j + 1]); vi[j] = pk_bf16(hi[2 * j], hi[2 * j + 1]); }
    *(u32x4*)(hrow + ((i ^ sw) * 16)) = vr;
    *(u32x4*)(hrow + 65536 + ((i ^ sw) * 16)) = vi;
    __builtin_amdgcn_sched_barrier(0);
  }
  WAIT_L0();
  BAR();
  u32x2 xs[2];
#pragma unroll
  for (int i = 0; i < 2; ++i) xs[i] = *(const u32x2*)(p.ax + (size_t)(t0 + 32 * i + (tid >> 5)) * 512 + cbk * 128 + (tid & 31) * 4);
  const u32x2 dsk = *(const u32x2*)(p.dskip + cbk * 128 + (tid & 31) * 4);
  BAR();
  BAR();
  k2_a_epi(p, sm, t0, cbk, xs, dsk);
}
DI void k2_a_gemm(const Params& p, char* sm, int jb, int cbk, int w) {
  constexpr int LA = CLA;
  const int tid = threadIdx.x, lane = tid & 63;
  const int i16 = lane & 15, g = lane >> 4;
  const int t0 = jb * 64;
  const int plane = (w >> 2) & 1, cb4 = w & 3;
  const int rot = (jb & 3) * 4;
  const char* cfb = p.ws + WS_CF + ((size_t)((plane * 16 + cbk * 4 + cb4) * 32) * 64 + lane) * 16;
  u32x4 cf[32];
  SBAR();
#pragma unroll
  for (int b = 0; b < LA; ++b) cf[b] = *(const u32x4*)(cfb + (((b >> 1) ^ rot) * 2 + (b & 1)) * 1024);
  SBAR();
  f32x4 acc[4][2] = {};
  u32x2 xs[2], dsk;
  {
    const int q4 = i16 >> 2, pp = lane & 3;
    const int s0 = (((q4 >> 1) & 1) << 2) | ((g & 1) << 1) | (q4 & 1);
    const int s1 = s0 ^ 2;
    const char* base = sm + plane * 65536 + (8 * g + q4) * 128 + (pp & 1) * 8;
    int o0[4], o1[4];
#pragma unroll
    for (int mt = 0; mt < 4; ++mt) { o0[mt] = ((2 * mt + (pp >> 1)) ^ s0) * 16; o1[mt] = 512 + ((2 * mt + (pp >> 1)) ^ s1) * 16; }
    u32x4 hfr[2][4];
#pragma unroll
    for (int mt = 0; mt < 4; ++mt) {
      const u32x2 a0 = tr_read(base + o0[mt]), a1 = tr_read(base + o1[mt]);
      hfr[0][mt] = u32x4{a0[0], a0[1], a1[0], a1[1]};
    }
#pragma unroll
    for (int b = 0; b < 16; ++b) {
#pragma unroll
      for (int e = 0; e < 2; ++e) {
        const int f = 2 * b + LA + e;
        if (f < 32) cf[f] = *(const u32x4*)(cfb + (((f >> 1) ^ rot) * 2 + (f & 1)) * 1024);
      }
      if (b == 14) {
#pragma unroll
        for (int i = 0; i < 2; ++i) xs[i] = *(const u32x2*)(p.ax + (size_t)(t0 + 32 * i + (tid >> 5)) * 512 + cbk * 128 + (tid & 31) * 4);
        dsk = *(const u32x2*)(p.dskip + cbk * 128 + (tid & 31) * 4);
      }
      if (b + 1 < 16) {
#pragma unroll
        for (int mt = 0; mt < 4; ++mt) {
          const u32x2 a0 = tr_read(base + o0[mt] + (b + 1) * 4096), a1 = tr_read(base + o1[mt] + (b + 1) * 4096);
          hfr[(b + 1) & 1][mt] = u32x4{a0[0], a0[1], a1[0], a1[1]};
        }
      }
#pragma unroll
      for (int mt = 0; mt < 4; ++mt) {
        acc[mt][0] = mfma16(cf[2 * b], hfr[b & 1][mt], acc[mt][0]);
        acc[mt][1] = mfma16(cf[2 * b + 1], hfr[b & 1][mt], acc[mt][1]);
      }
      INTERLEAVE(8);
    }
  }
  WAIT_L0();
  BAR();
  {
    char* ob = sm + plane * (64 * OSTR) + i16 * OSTR + (cb4 * 32 + 4 * g) * 4;
#pragma unroll
    for (int mt = 0; mt < 4; ++mt) {
      *(f32x4*)(ob + mt * 16 * OSTR) = acc[mt][0];
      *(f32x4*)(ob + mt * 16 * OSTR + 64) = acc[mt][1];
    }
  }
  WAIT_L0();
  BAR();
  k2_a_epi(p, sm, t0, cbk, xs, dsk);
}

DI void k2_b_epi(const Params& p, char* sm, int rb128, int nb) {
  const int tid = threadIdx.x;
#pragma unroll
  for (int i = 0; i < 2; ++i) {
    const int row = 64 * i + (tid >> 4);
    const char* ib = sm + row * OSTRB + (tid & 15) * 16;
    const f32x4 s = *(const f32x4*)(ib) + *(const f32x4*)(ib + 128 * OSTRB);
    const unsigned s01 = pk_bf16(s[0], s[1]);
    const unsigned s23 = pk_bf16(s[2], s[3]);
    f32x4 o; o[0] = bf_lo(s01); o[1] = bf_hi(s01); o[2] = bf_lo(s23); o[3] = bf_hi(s23);
    st16(p.out + (size_t)(rb128 * 128 + row) * 1024 + 512 + nb * 64 + (tid & 15) * 4, o);
  }
}
DI void k2_b_red(const Params& p, char* sm, int rb128, int nb) {
  const int tid = threadIdx.x;
  char* kvf = sm + 65536;
  u32x4 pa[2][8];
#pragma unroll
  for (int i = 0; i < 2; ++i)
#pragma unroll
    for (int R = 0; R < 8; ++R) pa[i][R] = *(const u32x4*)(p.ws + WS_PKV + ((size_t)R * 1024 + i * 512 + tid) * 16);
  float pz[8];
#pragma unroll
  for (int R = 0; R < 8; ++R) pz[R] = *(const float*)(p.ws + WS_PZ + ((size_t)R * 512 + tid) * 4);
  __builtin_amdgcn_sched_barrier(0);
#pragma unroll
  for (int i = 0; i < 2; ++i) {
    u32x4 o;
#pragma unroll
    for (int j = 0; j < 4; ++j) {
      float lo = 0.f, hi = 0.f;
#pragma unroll
      for (int R = 0; R < 8; ++R) { lo += bf_lo(pa[i][R][j]); hi += bf_hi(pa[i][R][j]); }
      o[j] = pk_bf16(lo, hi);
    }
    const int f = i * 512 + tid, head = f >> 5, e = (f >> 1) & 15, hh = f & 1;
    *(u32x4*)(kvf + (head * 64 + e + 32 * hh) * 16) = o;
  }
  {
    const float s = ((pz[0] + pz[1]) + (pz[2] + pz[3])) + ((pz[4] + pz[5]) + (pz[6] + pz[7]));
    const int head = tid >> 4, hh = (tid >> 3) & 1, jj = tid & 7;
    const u16 o = (u16)(pk_bf16(s, 0.f) & 0xffffu);
    *(u16*)(kvf + ((head * 64 + 16 + 32 * hh) * 8 + jj) * 2) = o;
    *(u16*)(kvf + ((head * 64 + 20 + 32 * hh) * 8 + jj) * 2) = o;
  }
  WAIT_L0();
  BAR();
  BAR();
  BAR();
  k2_b_epi(p, sm, rb128, nb);
}
constexpr int PLA = 8, PL0 = 4;
DI void k2_b_gemm(const Params& p, char* sm, int rb128, int nb, int w) {
  const int tid = threadIdx.x, lane = tid & 63;
  const int r = lane & 31, h = lane >> 5;
  const int rbw = (w >> 1) & 3, kh = w & 1;
  char* wos = sm;
  char* kvf = sm + 65536;
  const char* pqb = p.ws + WS_PQF + ((size_t)((rb128 * 4 + rbw) * 32 + kh * 16) * 64 + lane) * 16;
  u32x4 pq[16];
  {
    u32x4 wv[8];
#pragma unroll
    for (int i = 0; i < 8; ++i) wv[i] = *(const u32x4*)(p.ws + WS_WOF + (size_t)nb * 65536 + (i * 512 + tid - 512) * 16);
#pragma unroll
    for (int k = 0; k < PL0; ++k) pq[k] = *(const u32x4*)(pqb + k * 1024);
    __builtin_amdgcn_sched_barrier(0);
#pragma unroll
    for (int i = 0; i < 8; ++i) *(u32x4*)(wos + (i * 512 + tid - 512) * 16) = wv[i];
  }
  WAIT_L0();
  SBAR();
#pragma unroll
  for (int k = PL0; k < PLA; ++k) pq[k] = *(const u32x4*)(pqb + k * 1024);
  __builtin_amdgcn_sched_barrier(0);
  f32x16 acc0 = {}, acc1 = {};
  auto numden = [&](int i) {
    const u32x4 kf = *(const u32x4*)(kvf + ((kh * 16 + i) * 64 + lane) * 16);
    f32x16 nd = {};
    return mfma32(kf, pq[i], nd);
  };
  auto scale = [&](const f32x16& nd) {
    const float inv = __builtin_amdgcn_rcpf(nd[8] + 1e-6f);
    u32x4 of;
#pragma unroll
    for (int j = 0; j < 4; ++j) of[j] = pk_bf16(nd[2 * j] * inv, nd[2 * j + 1] * inv);
    return of;
  };
  f32x16 n0 = numden(0), n1 = numden(1);
#pragma unroll
  for (int gq = 0; gq < 8; ++gq) {
    if (2 * gq + PLA < 16) { pq[2 * gq + PLA] = *(const u32x4*)(pqb + (2 * gq + PLA) * 1024); pq[2 * gq + PLA + 1] = *(const u32x4*)(pqb + (2 * gq + PLA + 1) * 1024); }
    const u32x4 o0 = scale(n0), o1 = scale(n1);
    if (gq < 7) { n0 = numden(2 * gq + 2); n1 = numden(2 * gq + 3); }
    const int head = kh * 16 + 2 * gq;
    const u32x4 w0 = *(const u32x4*)(wos + ((head * 2 + 0) * 64 + lane) * 16);
    const u32x4 w1 = *(const u32x4*)(wos + ((head * 2 + 1) * 64 + lane) * 16);
    const u32x4 w2 = *(const u32x4*)(wos + ((head * 2 + 2) * 64 + lane) * 16);
    const u32x4 w3 = *(const u32x4*)(wos + ((head * 2 + 3) * 64 + lane) * 16);
    acc0 = mfma32(w0, o0, acc0);
    acc1 = mfma32(w1, o0, acc1);
    acc0 = mfma32(w2, o1, acc0);
    acc1 = mfma32(w3, o1, acc1);
    __builtin_amdgcn_sched_barrier(0);
  }
  WAIT_L0();
  BAR();
  {
    char* ob = sm + kh * (128 * OSTRB) + (rbw * 32 + r) * OSTRB + 4 * h * 4;
#pragma unroll
    for (int g4 = 0; g4 < 4; ++g4) {
      f32x4 a, b;
#pragma unroll
      for (int j = 0; j < 4; ++j) { a[j] = acc0[4 * g4 + j]; b[j] = acc1[4 * g4 + j]; }
      *(f32x4*)(ob + g4 * 32) = a;
      *(f32x4*)(ob + 128 + g4 * 32) = b;
    }
  }
  WAIT_L0();
  BAR();
  k2_b_epi(p, sm, rb128, nb);
}

__global__ __launch_bounds__(1024, 4) void k2(Params p) {
  extern __shared__ __attribute__((aligned(16))) char sm[];
  const int bid = blockIdx.x, xcd = bid & 7, j = bid >> 3;
  const int jj = j >> 1;
  const int w = __builtin_amdgcn_readfirstlane(threadIdx.x >> 6);
  if ((j & 1) == 0) {
    if (w < 8) k2_a_scan(p, sm, xcd * 4 + (jj >> 2), jj & 3); else k2_a_gemm(p, sm, xcd * 4 + (jj >> 2), jj & 3, w);
  } else {
    if (w < 8) k2_b_red(p, sm, xcd * 2 + (jj >> 3), jj & 7); else k2_b_gemm(p, sm, xcd * 2 + (jj >> 3), jj & 7, w);
  }
}

extern "C" void kernel_launch(void* const* d_in, const int* in_sizes, int n_in,
                              void* d_out, int out_size, void* d_ws, size_t ws_size,
                              hipStream_t stream) {
  Params p;
  p.ax = (const u16*)d_in[0]; p.lre = (const u16*)d_in[1]; p.lim = (const u16*)d_in[2];
  p.bre = (const u16*)d_in[3]; p.bim = (const u16*)d_in[4]; p.cre = (const u16*)d_in[5]; p.cim = (const u16*)d_in[6];
  p.ldt = (const u16*)d_in[7]; p.dskip = (const u16*)d_in[8]; p.bx = (const u16*)d_in[9];
  p.wqkv = (const u16*)d_in[10]; p.wo = (const u16*)d_in[11];
  p.out = (float*)d_out; p.ws = (char*)d_ws;
  (void)hipFuncSetAttribute((const void*)k1, hipFuncAttributeMaxDynamicSharedMemorySize, K1_LDS);
  (void)hipFuncSetAttribute((const void*)k2, hipFuncAttributeMaxDynamicSharedMemorySize, K2_LDS);
  k1<<<256, 1024, K1_LDS, stream>>>(p);
  k2<<<256, 1024, K2_LDS, stream>>>(p);
}
```
